```python
import torch

def _softmax(x, axis=-1):
    return torch.softmax(x, dim=axis)
def _gelu(x):
    return torch.nn.functional.gelu(x, approximate="tanh")
def _silu(x):
    return torch.nn.functional.silu(x)
def _sigmoid(x):
    return torch.sigmoid(x)
def _layernorm(x, axis=-1, eps=1e-5):
    m = x.mean(dim=axis, keepdim=True)
    v = x.var(dim=axis, keepdim=True, unbiased=False)
    return (x - m) / torch.sqrt(v + eps)
def _scatter_add(tgt, idx, src, axis=0):
    return tgt.clone().index_add_(axis, idx, src)
def _segment_sum(src, seg, n):
    out = torch.zeros((n,) + src.shape[1:], dtype=src.dtype)
    return out.index_add_(0, seg, src)
def _pad(x, pw):
    flat = tuple(v for pair in reversed(pw) for v in pair)
    return torch.nn.functional.pad(x, flat)

kernel_name = 'gather_scatter__edge_featured_mpnn_gatedgcn__graph'

def setup_inputs():

    _w = (lambda a: (lambda t: t.to(torch.bfloat16) if t.is_floating_point() else t)(torch.as_tensor(a)))
    N, deg, D, De = 50000, 16, 64, max(4, 1)
    E = N * deg
    return {
        "x": _w((lambda *s: torch.randn(*s, dtype=torch.bfloat16) * 0.4)(N, D)),
        "e_attr": _w((lambda *s: torch.randn(*s, dtype=torch.bfloat16) * 0.8)(E, De)),
        "src": _w((lambda lo, hi, *s: torch.randint(lo, hi, s))(0, N, E).to(torch.int64)),
        "dst": _w((lambda lo, hi, *s: torch.randint(lo, hi, s))(0, N, E).to(torch.int64)),
        "w_a": _w((lambda *s: torch.randn(*s, dtype=torch.bfloat16) * 0.8)(D, D)),
        "w_b": _w((lambda *s: torch.randn(*s, dtype=torch.bfloat16) * 0.4)(D, D)),
        "w_c": _w((lambda *s: torch.randn(*s, dtype=torch.bfloat16) * 0.8)(De, D)),
        "w_u": _w((lambda *s: torch.randn(*s, dtype=torch.bfloat16) * 0.05)(D, D)),
        "w_v": _w((lambda *s: torch.randn(*s, dtype=torch.bfloat16) * 0.05)(D, D)),
    }

def reference(x, e_attr, src, dst, w_a, w_b, w_c, w_u, w_v):
    N, D = x.shape
    hv = x @ w_v
    g = x[dst] @ w_a + x[src] @ w_b + e_attr @ w_c
    eta = _sigmoid(g)
    denom = _segment_sum(eta, dst, N) + 1e-6
    eta_n = eta / denom[dst]
    agg = _segment_sum(eta_n * hv[src], dst, N)
    z = x @ w_u + agg
    relu_z = torch.where(z > 0, z, 0 * z)
    return x + relu_z
```

```cpp
#include <hip/hip_runtime.h>
#include <hip/hip_bf16.h>
#include <stdint.h>

__device__ __forceinline__ float bf2f(uint16_t v){ return __uint_as_float(((uint32_t)v)<<16); }

__global__ void k_node(const uint16_t* x, const uint16_t* wa, const uint16_t* wb, const uint16_t* wv, const uint16_t* wu,
                       float* A, float* B, float* HV, float* U, int N){
  int n = blockIdx.x; int c = threadIdx.x;
  float a=0,b=0,v=0,u=0;
  for(int k=0;k<64;k++){ float xv=bf2f(x[n*64+k]);
    a+=xv*bf2f(wa[k*64+c]); b+=xv*bf2f(wb[k*64+c]); v+=xv*bf2f(wv[k*64+c]); u+=xv*bf2f(wu[k*64+c]); }
  A[n*64+c]=a;B[n*64+c]=b;HV[n*64+c]=v;U[n*64+c]=u;
}
__global__ void k_edge(const uint16_t* ea, const int* src, const int* dst, const uint16_t* wc,
                       const float* A, const float* B, const float* HV, float* den, float* num, int E){
  int e = blockIdx.x*4 + (threadIdx.x>>6); int c = threadIdx.x&63;
  if(e>=E) return;
  int s=src[e], d=dst[e];
  float g=A[d*64+c]+B[s*64+c];
  for(int k=0;k<4;k++) g+=bf2f(ea[e*4+k])*bf2f(wc[k*64+c]);
  float eta=1.f/(1.f+__expf(-g));
  atomicAdd(&den[d*64+c],eta);
  atomicAdd(&num[d*64+c],eta*HV[s*64+c]);
}
__global__ void k_final(const uint16_t* x,const float* U,const float* den,const float* num,__hip_bfloat16* out,int n){
  int i=blockIdx.x*256+threadIdx.x; if(i>=n) return;
  float z=U[i]+num[i]/(den[i]+1e-6f);
  z=z>0?z:0.f;
  out[i]=__float2bfloat16(bf2f(x[i])+z);
}

extern "C" void kernel_launch(void* const* d_in, const int* in_sizes, int n_in,
                              void* d_out, int out_size, void* d_ws, size_t ws_size,
                              hipStream_t stream) {
  const uint16_t* x=(const uint16_t*)d_in[0]; const uint16_t* ea=(const uint16_t*)d_in[1];
  const int* src=(const int*)d_in[2]; const int* dst=(const int*)d_in[3];
  const uint16_t* wa=(const uint16_t*)d_in[4]; const uint16_t* wb=(const uint16_t*)d_in[5];
  const uint16_t* wc=(const uint16_t*)d_in[6]; const uint16_t* wu=(const uint16_t*)d_in[7]; const uint16_t* wv=(const uint16_t*)d_in[8];
  int N=in_sizes[0]/64, E=in_sizes[2];
  size_t nd=(size_t)N*64;
  float* A=(float*)d_ws; float* B=A+nd; float* HV=B+nd; float* U=HV+nd; float* den=U+nd; float* num=den+nd;
  hipMemsetAsync(den,0,nd*2*sizeof(float),stream);
  k_node<<<N,64,0,stream>>>(x,wa,wb,wv,wu,A,B,HV,U,N);
  k_edge<<<(E+3)/4,256,0,stream>>>(ea,src,dst,wc,A,B,HV,den,num,E);
  k_final<<<(nd+255)/256,256,0,stream>>>(x,U,den,num,(__hip_bfloat16*)d_out,(int)nd);
}
```

```cpp
#include <hip/hip_runtime.h>
#include <hip/hip_bf16.h>
#include <stdint.h>

#define DI __device__ __forceinline__

typedef __attribute__((ext_vector_type(8))) __bf16 bf16x8_t;
typedef __attribute__((ext_vector_type(2))) __bf16 bf16x2_t;
typedef __attribute__((ext_vector_type(4))) float f32x4;
typedef __attribute__((ext_vector_type(2))) float f32x2;
typedef __attribute__((ext_vector_type(4))) uint32_t u32x4;
typedef __attribute__((ext_vector_type(2))) uint32_t u32x2;

namespace {

constexpr int NB     = 512;
constexpr int NCHUNK = 256;
constexpr int LSTR   = 520;
constexpr int CAP    = 2048;
constexpr int NWAVE  = 4;
constexpr int MAXWIN = 64;
constexpr int EPT    = 13;

DI f32x4 mfma16(u32x4 a, u32x4 b, f32x4 c) {
  return __builtin_amdgcn_mfma_f32_16x16x32_bf16(__builtin_bit_cast(bf16x8_t, a),
                                                 __builtin_bit_cast(bf16x8_t, b), c, 0, 0, 0);
}
DI uint32_t pack_bf16(float lo, float hi) {
  f32x2 v = {lo, hi};
  bf16x2_t r = __builtin_convertvector(v, bf16x2_t);
  return __builtin_bit_cast(uint32_t, r);
}
DI float bf_lo(uint32_t v) { return __uint_as_float(v << 16); }
DI float bf_hi(uint32_t v) { return __uint_as_float(v & 0xFFFF0000u); }

DI uint32_t block_excl_scan256(uint32_t v, uint32_t* wsum, uint32_t* total) {
  const int lane = threadIdx.x & 63, wave = threadIdx.x >> 6;
  uint32_t inc = v;
#pragma unroll
  for (int o = 1; o < 64; o <<= 1) {
    uint32_t y = __shfl_up(inc, o, 64);
    if (lane >= o) inc += y;
  }
  if (lane == 63) wsum[wave] = inc;
  __syncthreads();
  uint32_t pre = 0, tot = 0;
#pragma unroll
  for (int k = 0; k < 4; ++k) {
    uint32_t s = wsum[k];
    if (k < wave) pre += s;
    tot += s;
  }
  *total = tot;
  return pre + inc - v;
}

__global__ __launch_bounds__(256, 4) void k1_sort(
    const int* __restrict__ src, const int* __restrict__ dst, const u32x2* __restrict__ eattr,
    u32x4* __restrict__ grec, uint16_t* __restrict__ loff,
    const uint16_t* __restrict__ w_a, const uint16_t* __restrict__ w_b,
    const uint16_t* __restrict__ w_c, const uint16_t* __restrict__ w_u,
    const uint16_t* __restrict__ w_v, u32x4* __restrict__ wfrag, u32x2* __restrict__ wcfrag,
    int E, int CH, int NPB, uint32_t magic) {
  __shared__ uint32_t hist[NB];
  __shared__ uint32_t wsum[4];
  const int t = threadIdx.x;
  const int w = blockIdx.x;

  if (w >= NCHUNK) {
#pragma unroll 1
    for (int f = t; f < 4 * 8 * 64; f += 256) {
      const int m = f >> 9, bs = (f >> 6) & 7, lane = f & 63;
      const int blk = bs >> 1, s = bs & 1, i = lane & 15, q = lane >> 4;
      const uint16_t* W = (m == 0) ? w_b : (m == 1) ? w_v : (m == 2) ? w_a : w_u;
      const int c = 4 * i + blk;
      u32x4 r;
#pragma unroll
      for (int jj = 0; jj < 4; ++jj) {
        const int k0 = 32 * s + 8 * q + 2 * jj;
        uint32_t lo = W[k0 * 64 + c], hi = W[(k0 + 1) * 64 + c];
        r[jj] = lo | (hi << 16);
      }
      wfrag[f] = r;
    }
#pragma unroll 1
    for (int f = t; f < 4 * 64; f += 256) {
      const int blk = f >> 6, lane = f & 63, i = lane & 15, q = lane >> 4;
      const int c = 4 * i + blk;
      u32x2 r = {0u, 0u};
      if (q == 0) {
        r[0] = (uint32_t)w_c[0 * 64 + c] | ((uint32_t)w_c[1 * 64 + c] << 16);
        r[1] = (uint32_t)w_c[2 * 64 + c] | ((uint32_t)w_c[3 * 64 + c] << 16);
      }
      wcfrag[f] = r;
    }
    return;
  }

  hist[t] = 0;
  hist[t + 256] = 0;
  const int base = w * CH;
  int len = E - base;
  len = len < 0 ? 0 : (len > CH ? CH : len);

  int d[EPT], s[EPT];
  u32x2 ea[EPT];
#pragma unroll
  for (int k = 0; k < EPT; ++k) {
    const int idx = t + k * 256;
    int e = base + (idx < len ? idx : 0);
    e = e < E ? e : 0;
    d[k] = dst[e];
    s[k] = src[e];
    ea[k] = eattr[e];
  }
  __syncthreads();
  uint32_t br[EPT];
#pragma unroll
  for (int k = 0; k < EPT; ++k) {
    const int idx = t + k * 256;
    const uint32_t b = magic ? __umulhi((uint32_t)d[k], magic) : (uint32_t)d[k] / (uint32_t)NPB;
    const uint32_t rk = atomicAdd(&hist[b], idx < len ? 1u : 0u);
    br[k] = (b << 16) | rk;
  }
  __syncthreads();
  const uint32_t v0 = hist[2 * t], v1 = hist[2 * t + 1];
  uint32_t total;
  const uint32_t ex = block_excl_scan256(v0 + v1, wsum, &total);
  hist[2 * t] = ex;
  hist[2 * t + 1] = ex + v0;
  reinterpret_cast<uint32_t*>(loff + (size_t)w * LSTR)[t] = ex | ((ex + v0) << 16);
  if (t == 0) loff[(size_t)w * LSTR + NB] = (uint16_t)len;
  __syncthreads();
#pragma unroll
  for (int k = 0; k < EPT; ++k) {
    const int idx = t + k * 256;
    if (idx < len) {
      const uint32_t pos = hist[br[k] >> 16] + (br[k] & 0xFFFFu);
      u32x4 r = {ea[k][0], ea[k][1], (uint32_t)s[k], (uint32_t)d[k]};
      grec[(size_t)base + pos] = r;
    }
  }
}

struct TileIn {
  u32x4 xg[2][2];
  u32x4 asel[2];
  u32x4 ared;
};

__global__ __launch_bounds__(256, 2) void k2_main(
    const uint16_t* __restrict__ x, const u32x4* __restrict__ grec,
    const uint16_t* __restrict__ loff, const u32x4* __restrict__ wfrag,
    const u32x2* __restrict__ wcfrag, uint16_t* __restrict__ out, float* __restrict__ part,
    int N, int CH, int NPB, int WS, int nwin) {
  __shared__ u32x4 recs[CAP];
  __shared__ uint32_t key[CAP];
  __shared__ __attribute__((aligned(16))) uint32_t perm[CAP + 4 * MAXWIN + 64];
  __shared__ u32x4 wau[2 * 8 * 64];
  __shared__ uint32_t wcnt[MAXWIN], woff[MAXWIN];
  __shared__ uint32_t wsum[4];

  const int t = threadIdx.x;
  const int b = blockIdx.x;
  const int lane = t & 63, wave = t >> 6, i = lane & 15, q = lane >> 4;
  const int nodebase = b * NPB;
  int node_end = nodebase + NPB;
  node_end = node_end > N ? N : node_end;

  const uint32_t s0 = loff[(size_t)t * LSTR + b];
  const uint32_t s1 = loff[(size_t)t * LSTR + b + 1];
  const uint32_t slen = s1 - s0;
#pragma unroll
  for (int k = 0; k < 4; ++k) wau[t + 256 * k] = wfrag[2 * 512 + t + 256 * k];
  uint32_t total;
  const uint32_t so = block_excl_scan256(slen, wsum, &total);
  const u32x4* gseg = grec + (size_t)t * CH + s0;

  u32x4 wb[4][2], wv[4][2];
#pragma unroll
  for (int blk = 0; blk < 4; ++blk) {
#pragma unroll
    for (int s = 0; s < 2; ++s) {
      wb[blk][s] = wfrag[(0 * 8 + blk * 2 + s) * 64 + lane];
      wv[blk][s] = wfrag[(1 * 8 + blk * 2 + s) * 64 + lane];
    }
  }

  for (uint32_t bstart = 0;; bstart += CAP) {
    const bool first = (bstart == 0);
    const bool last = (bstart + CAP >= total);
    if (t < MAXWIN) wcnt[t] = 0;
    __syncthreads();
    {
      const uint32_t k0 = (bstart > so) ? (bstart - so) : 0u;
      uint32_t k1 = slen;
      if (so + k1 > bstart + CAP) k1 = (bstart + CAP > so) ? (bstart + CAP - so) : 0u;
      for (uint32_t k = k0; k < k1; k += 4) {
        u32x4 r[4];
#pragma unroll
        for (int u = 0; u < 4; ++u)
          if (k + u < k1) r[u] = gseg[k + u];
#pragma unroll
        for (int u = 0; u < 4; ++u)
          if (k + u < k1) {
            const uint32_t pos = so + k + u - bstart;
            recs[pos] = r[u];
            const uint32_t dl = r[u][3] - (uint32_t)nodebase;
            const uint32_t win = dl / (uint32_t)WS;
            const uint32_t dlw = dl - win * (uint32_t)WS;
            const uint32_t rk = atomicAdd(&wcnt[win], 1u);
            key[pos] = rk | (win << 16) | (dlw << 24);
          }
      }
    }
    __syncthreads();
    if (t == 0) {
      uint32_t acc = 0;
      for (int k = 0; k < nwin; ++k) {
        woff[k] = acc;
        acc += (wcnt[k] + 3u) & ~3u;
      }
    }
    __syncthreads();
    {
      const uint32_t bcount = (total - bstart) > (uint32_t)CAP ? (uint32_t)CAP : (total - bstart);
      for (uint32_t pos = t; pos < bcount; pos += 256) {
        const uint32_t k = key[pos];
        perm[woff[(k >> 16) & 0xFFu] + (k & 0xFFFFu)] = pos | (k & 0xFF000000u);
      }
    }
    __syncthreads();

    for (int win = wave; win < nwin; win += NWAVE) {
      const int n0 = nodebase + win * WS;
      int wsize = node_end - n0;
      wsize = wsize > WS ? WS : wsize;
      if (wsize <= 0) break;
      const uint32_t cnt = wcnt[win];
      const uint32_t base = woff[win];

      const int nrow = n0 + (i < wsize ? i : wsize - 1);
      const u32x4* xrow = reinterpret_cast<const u32x4*>(x + (size_t)nrow * 64 + 8 * q);
      const u32x4 xa0 = xrow[0], xa1 = xrow[4];
      u32x4 bsel[4];
#pragma unroll
      for (int blk = 0; blk < 4; ++blk) {
        f32x4 a = {0.f, 0.f, 0.f, 0.f};
        a = mfma16(xa0, wau[(blk * 2 + 0) * 64 + lane], a);
        a = mfma16(xa1, wau[(blk * 2 + 1) * 64 + lane], a);
        const u32x2 wcf = wcfrag[blk * 64 + lane];
        bsel[blk] = (u32x4){pack_bf16(a[0], a[1]), pack_bf16(a[2], a[3]), wcf[0], wcf[1]};
      }

      f32x4 den[4], num[4];
#pragma unroll
      for (int blk = 0; blk < 4; ++blk) {
        den[blk] = (f32x4){0.f, 0.f, 0.f, 0.f};
        num[blk] = (f32x4){0.f, 0.f, 0.f, 0.f};
      }
      if (!first) {
#pragma unroll
        for (int blk = 0; blk < 4; ++blk)
#pragma unroll
          for (int r = 0; r < 4; ++r) {
            const int nl = 4 * q + r;
            if (nl < wsize) {
              const float* pp = part + (size_t)(n0 + nl) * 128 + 4 * i + blk;
              den[blk][r] = pp[0];
              num[blk][r] = pp[64];
            }
          }
      }

      const int ntiles = (int)((cnt + 31u) >> 5);

      auto fetch = [&](int tt, TileIn& T) {
#pragma unroll
        for (int s2 = 0; s2 < 2; ++s2) {
          const uint32_t eidx = 32u * tt + 16u * s2 + i;
          const bool valid = eidx < cnt;
          const uint32_t pe = perm[base + eidx];
          const uint32_t pos = valid ? (pe & 0xFFFFFFu) : 0u;
          const u32x4 rec = recs[pos];
          const uint32_t sn = valid ? rec[2] : 0u;
          const u32x4* xp = reinterpret_cast<const u32x4*>(x + (size_t)sn * 64 + 8 * q);
          T.xg[s2][0] = xp[0];
          T.xg[s2][1] = xp[4];
          const uint32_t dl = valid ? (pe >> 24) : 255u;
          const uint32_t u = dl - 4u * q;
          const uint32_t one = 0x3F80u << ((u & 1u) << 4);
          const bool eq = valid && (q == 0);
          T.asel[s2] = (u32x4){(u >> 1) == 0u ? one : 0u, (u >> 1) == 1u ? one : 0u,
                               eq ? rec[0] : 0u, eq ? rec[1] : 0u};
        }
        u32x4 ar = {0u, 0u, 0u, 0u};
#pragma unroll
        for (int s2 = 0; s2 < 2; ++s2) {
          const uint32_t e4 = 32u * tt + 16u * s2 + 4u * q;
          const u32x4 pv = *reinterpret_cast<const u32x4*>(&perm[base + e4]);
#pragma unroll
          for (int jj = 0; jj < 4; ++jj) {
            const bool m = (e4 + jj < cnt) && ((pv[jj] >> 24) == (uint32_t)i);
            ar[2 * s2 + (jj >> 1)] |= m ? (0x3F80u << (16 * (jj & 1))) : 0u;
          }
        }
        T.ared = ar;
      };

      TileIn cur, nxt;
      if (ntiles > 0) fetch(0, nxt);
      for (int tt = 0; tt < ntiles; ++tt) {
        cur = nxt;
        if (tt + 1 < ntiles) fetch(tt + 1, nxt);
#pragma unroll
        for (int blk = 0; blk < 4; ++blk) {
          u32x4 eb, pb;
#pragma unroll
          for (int s2 = 0; s2 < 2; ++s2) {
            const f32x4 zero = {0.f, 0.f, 0.f, 0.f};
            f32x4 g = mfma16(cur.asel[s2], bsel[blk], zero);
            g = mfma16(cur.xg[s2][0], wb[blk][0], g);
            g = mfma16(cur.xg[s2][1], wb[blk][1], g);
            f32x4 v = mfma16(cur.xg[s2][0], wv[blk][0], zero);
            v = mfma16(cur.xg[s2][1], wv[blk][1], v);
            float eta[4], p[4];
#pragma unroll
            for (int r = 0; r < 4; ++r) {
              eta[r] = __builtin_amdgcn_rcpf(1.f + __builtin_amdgcn_exp2f(g[r] * -1.44269504088896f));
              p[r] = eta[r] * v[r];
            }
            eb[2 * s2 + 0] = pack_bf16(eta[0], eta[1]);
            eb[2 * s2 + 1] = pack_bf16(eta[2], eta[3]);
            pb[2 * s2 + 0] = pack_bf16(p[0], p[1]);
            pb[2 * s2 + 1] = pack_bf16(p[2], p[3]);
          }
          den[blk] = mfma16(cur.ared, eb, den[blk]);
          num[blk] = mfma16(cur.ared, pb, num[blk]);
        }
      }

      if (last) {
        f32x4 uu[4];
        const u32x4 xb0 = xrow[0], xb1 = xrow[4];
#pragma unroll
        for (int blk = 0; blk < 4; ++blk) {
          f32x4 a = {0.f, 0.f, 0.f, 0.f};
          a = mfma16(xb0, wau[(8 + blk * 2 + 0) * 64 + lane], a);
          a = mfma16(xb1, wau[(8 + blk * 2 + 1) * 64 + lane], a);
          uu[blk] = a;
        }
#pragma unroll
        for (int r = 0; r < 4; ++r) {
          const int nl = 4 * q + r;
          if (nl < wsize) {
            const size_t node = (size_t)(n0 + nl);
            const u32x2 xr = *reinterpret_cast<const u32x2*>(x + node * 64 + 4 * i);
            float o[4];
#pragma unroll
            for (int blk = 0; blk < 4; ++blk) {
              const uint32_t xw = xr[blk >> 1];
              const float xv = (blk & 1) ? bf_hi(xw) : bf_lo(xw);
              float z = uu[blk][r] + num[blk][r] * __builtin_amdgcn_rcpf(den[blk][r] + 1e-6f);
              z = z > 0.f ? z : 0.f;
              o[blk] = xv + z;
            }
            const u32x2 ov = {pack_bf16(o[0], o[1]), pack_bf16(o[2], o[3])};
            *reinterpret_cast<u32x2*>(out + node * 64 + 4 * i) = ov;
          }
        }
      } else {
#pragma unroll
        for (int blk = 0; blk < 4; ++blk)
#pragma unroll
          for (int r = 0; r < 4; ++r) {
            const int nl = 4 * q + r;
            if (nl < wsize) {
              float* pp = part + (size_t)(n0 + nl) * 128 + 4 * i + blk;
              pp[0] = den[blk][r];
              pp[64] = num[blk][r];
            }
          }
      }
    }
    if (last) break;
    __syncthreads();
  }
}

}

extern "C" void kernel_launch(void* const* d_in, const int* in_sizes, int n_in, void* d_out,
                              int out_size, void* d_ws, size_t ws_size, hipStream_t stream) {
  const uint16_t* x  = (const uint16_t*)d_in[0];
  const u32x2* ea    = (const u32x2*)d_in[1];
  const int* src     = (const int*)d_in[2];
  const int* dst     = (const int*)d_in[3];
  const uint16_t* wa = (const uint16_t*)d_in[4];
  const uint16_t* wb = (const uint16_t*)d_in[5];
  const uint16_t* wc = (const uint16_t*)d_in[6];
  const uint16_t* wu = (const uint16_t*)d_in[7];
  const uint16_t* wv = (const uint16_t*)d_in[8];
  const int N = in_sizes[0] / 64;
  const int E = in_sizes[2];

  const int CH = (E + NCHUNK - 1) / NCHUNK;
  const int NPB = (N + NB - 1) / NB;
  int nwin = NWAVE;
  while ((NPB + nwin - 1) / nwin > 16) nwin += NWAVE;
  const int WS = (NPB + nwin - 1) / nwin;

  uint32_t magic = (uint32_t)((0x100000000ull / (uint64_t)NPB) + 1ull);
  for (int dd = 0; dd < N; ++dd) {
    if ((uint32_t)(((uint64_t)(uint32_t)dd * magic) >> 32) != (uint32_t)(dd / NPB)) { magic = 0; break; }
  }

  char* ws = (char*)d_ws;
  size_t off = 0;
  u32x4* grec = (u32x4*)(ws + off);        off += (size_t)E * 16;
  off = (off + 255) & ~(size_t)255;
  uint16_t* loff = (uint16_t*)(ws + off);  off += (size_t)NCHUNK * LSTR * 2;
  off = (off + 255) & ~(size_t)255;
  u32x4* wfrag = (u32x4*)(ws + off);       off += 4 * 8 * 64 * 16;
  u32x2* wcfrag = (u32x2*)(ws + off);      off += 4 * 64 * 8;
  off = (off + 255) & ~(size_t)255;
  float* part = (float*)(ws + off);

  k1_sort<<<NCHUNK + 1, 256, 0, stream>>>(src, dst, ea, grec, loff, wa, wb, wc, wu, wv, wfrag,
                                          wcfrag, E, CH, NPB, magic);
  k2_main<<<NB, 256, 0, stream>>>(x, grec, loff, wfrag, wcfrag, (uint16_t*)d_out, part, N, CH,
                                  NPB, WS, nwin);
}
```

```cpp
#include <hip/hip_runtime.h>
#include <hip/hip_bf16.h>
#include <stdint.h>

#define DI __device__ __forceinline__

typedef __attribute__((ext_vector_type(8))) __bf16 bf16x8_t;
typedef __attribute__((ext_vector_type(2))) __bf16 bf16x2_t;
typedef __attribute__((ext_vector_type(4))) float f32x4;
typedef __attribute__((ext_vector_type(2))) float f32x2;
typedef __attribute__((ext_vector_type(4))) uint32_t u32x4;
typedef __attribute__((ext_vector_type(2))) uint32_t u32x2;

namespace {

constexpr int NB      = 512;
constexpr int NWIN    = 8;
constexpr int NWT     = NB * NWIN;
constexpr int NCHUNK  = 256;
constexpr int CAPREC  = 2048;
constexpr int CAPPAD  = CAPREC + 32 * NWIN;
constexpr int NWAVE   = 4;
constexpr int EPT     = 13;
constexpr int NPACKWG = 9;
constexpr int PBU     = 4;

DI f32x4 mfma16(u32x4 a, u32x4 b, f32x4 c) {
  return __builtin_amdgcn_mfma_f32_16x16x32_bf16(__builtin_bit_cast(bf16x8_t, a),
                                                 __builtin_bit_cast(bf16x8_t, b), c, 0, 0, 0);
}
DI uint32_t pack_bf16(float lo, float hi) {
  f32x2 v = {lo, hi};
  bf16x2_t r = __builtin_convertvector(v, bf16x2_t);
  return __builtin_bit_cast(uint32_t, r);
}
DI float bf_lo(uint32_t v) { return __uint_as_float(v << 16); }
DI float bf_hi(uint32_t v) { return __uint_as_float(v & 0xFFFF0000u); }

DI uint32_t wave_incl_scan(uint32_t v, int lane) {
#pragma unroll
  for (int o = 1; o < 64; o <<= 1) {
    uint32_t y = __shfl_up(v, o, 64);
    if (lane >= o) v += y;
  }
  return v;
}

__global__ __launch_bounds__(256, 4) void k1_sort(
    const int* __restrict__ src, const int* __restrict__ dst, const u32x2* __restrict__ eattr,
    u32x4* __restrict__ grec, u32x4* __restrict__ desc,
    const uint16_t* __restrict__ w_a, const uint16_t* __restrict__ w_b,
    const uint16_t* __restrict__ w_c, const uint16_t* __restrict__ w_u,
    const uint16_t* __restrict__ w_v, u32x4* __restrict__ wfrag, u32x2* __restrict__ wcfrag,
    int E, int CH, int NPB, int WS, uint32_t magicB, uint32_t magicW) {
  __shared__ uint32_t hist[NWT];
  __shared__ uint32_t wsum[4];
  __shared__ u32x4 srec[256 * EPT];
  const int t = threadIdx.x;
  const int w = blockIdx.x;

  if (w >= NCHUNK) {
    const int g = (w - NCHUNK) * 256 + t;
    if (g < 4 * 8 * 64) {
      const int f = g;
      const int m = f >> 9, bs = (f >> 6) & 7, lane = f & 63;
      const int blk = bs >> 1, s = bs & 1, i = lane & 15, q = lane >> 4;
      const uint16_t* W = (m == 0) ? w_b : (m == 1) ? w_v : (m == 2) ? w_a : w_u;
      const int c = 4 * i + blk;
      u32x4 r;
#pragma unroll
      for (int jj = 0; jj < 4; ++jj) {
        const int k0 = 32 * s + 8 * q + 2 * jj;
        uint32_t lo = W[k0 * 64 + c], hi = W[(k0 + 1) * 64 + c];
        r[jj] = lo | (hi << 16);
      }
      wfrag[f] = r;
    } else if (g < 4 * 8 * 64 + 4 * 64) {
      const int f = g - 4 * 8 * 64;
      const int blk = f >> 6, lane = f & 63, i = lane & 15, q = lane >> 4;
      const int c = 4 * i + blk;
      u32x2 r = {0u, 0u};
      if (q == 0) {
        r[0] = (uint32_t)w_c[0 * 64 + c] | ((uint32_t)w_c[1 * 64 + c] << 16);
        r[1] = (uint32_t)w_c[2 * 64 + c] | ((uint32_t)w_c[3 * 64 + c] << 16);
      }
      wcfrag[f] = r;
    }
    return;
  }

#pragma unroll
  for (int k = 0; k < NWT / 256 / 4; ++k)
    reinterpret_cast<u32x4*>(hist)[t + 256 * k] = (u32x4){0u, 0u, 0u, 0u};
  const int base = w * CH;
  int len = E - base;
  len = len < 0 ? 0 : (len > CH ? CH : len);

  int d[EPT], s[EPT];
  u32x2 ea[EPT];
#pragma unroll
  for (int k = 0; k < EPT; ++k) {
    const int idx = t + k * 256;
    int e = base + (idx < len ? idx : 0);
    e = e < E ? e : 0;
    d[k] = dst[e];
    s[k] = src[e];
    ea[k] = eattr[e];
  }
  __syncthreads();
  uint32_t br[EPT];
#pragma unroll
  for (int k = 0; k < EPT; ++k) {
    const int idx = t + k * 256;
    const uint32_t dd = (uint32_t)d[k];
    const uint32_t b = magicB ? __umulhi(dd, magicB) : dd / (uint32_t)NPB;
    const uint32_t dl = dd - b * (uint32_t)NPB;
    const uint32_t wn = magicW ? __umulhi(dl, magicW) : dl / (uint32_t)WS;
    const uint32_t f = b * NWIN + wn;
    const uint32_t rk = atomicAdd(&hist[f], idx < len ? 1u : 0u);
    br[k] = (f << 16) | rk;
  }
  __syncthreads();
  {
    const int lane = t & 63, wave = t >> 6;
    u32x4 h[4];
#pragma unroll
    for (int k = 0; k < 4; ++k) h[k] = reinterpret_cast<u32x4*>(hist)[4 * t + k];
    uint32_t run = 0;
    uint32_t ex[16];
#pragma unroll
    for (int k = 0; k < 16; ++k) {
      ex[k] = run;
      run += h[k >> 2][k & 3];
    }
    const uint32_t inc = wave_incl_scan(run, lane);
    if (lane == 63) wsum[wave] = inc;
    __syncthreads();
    uint32_t pre = 0;
#pragma unroll
    for (int k = 0; k < 4; ++k)
      if (k < wave) pre += wsum[k];
    pre += inc - run;
#pragma unroll
    for (int k = 0; k < 4; ++k) {
      u32x4 o;
#pragma unroll
      for (int j = 0; j < 4; ++j) o[j] = pre + ex[4 * k + j];
      reinterpret_cast<u32x4*>(hist)[4 * t + k] = o;
    }
    u32x4 p0, p1;
#pragma unroll
    for (int j = 0; j < 4; ++j) {
      p0[j] = (pre + ex[2 * j]) | ((pre + ex[2 * j + 1]) << 16);
      p1[j] = (pre + ex[8 + 2 * j]) | ((pre + ex[8 + 2 * j + 1]) << 16);
    }
    u32x4* d0 = desc + ((size_t)(2 * t) * NCHUNK + w) * 2;
    u32x4* d1 = desc + ((size_t)(2 * t + 1) * NCHUNK + w) * 2;
    d0[0] = p0;
    d0[1] = (u32x4){pre + ex[8], 0u, 0u, 0u};
    d1[0] = p1;
    d1[1] = (u32x4){pre + run, 0u, 0u, 0u};
  }
  __syncthreads();
#pragma unroll
  for (int k = 0; k < EPT; ++k) {
    const int idx = t + k * 256;
    if (idx < len) {
      const uint32_t pos = hist[br[k] >> 16] + (br[k] & 0xFFFFu);
      u32x4 r = {ea[k][0], ea[k][1], (uint32_t)s[k], (uint32_t)d[k]};
      srec[pos] = r;
    }
  }
  __syncthreads();
#pragma unroll
  for (int k = 0; k < EPT; ++k) {
    const int idx = t + k * 256;
    if (idx < len) grec[(size_t)base + idx] = srec[idx];
  }
}

struct TileIn {
  u32x4 xg[2][2];
};

__global__ __launch_bounds__(256, 2) void k2_main(
    const uint16_t* __restrict__ x, const u32x4* __restrict__ grec,
    const u32x4* __restrict__ desc, const u32x4* __restrict__ wfrag,
    const u32x2* __restrict__ wcfrag, uint16_t* __restrict__ out, float* __restrict__ part,
    int N, int CH, int NPB, int WS, uint32_t magicW) {
  __shared__ u32x4 recs[CAPPAD];
  __shared__ __attribute__((aligned(16))) uint8_t dlw8[CAPPAD + 64];
  __shared__ u32x4 wau[2 * 8 * 64];
  __shared__ uint32_t dtab[NWIN * 256];
  __shared__ uint32_t wsum[NWIN][4];
  __shared__ u32x2 wtab[NWIN];
  __shared__ u32x2 segtab[NCHUNK];
  __shared__ uint32_t wctr;

  const int t = threadIdx.x;
  const int b = blockIdx.x;
  const int lane = t & 63, wave = t >> 6, i = lane & 15, q = lane >> 4;
  const int nodebase = b * NPB;
  int node_end = nodebase + NPB;
  node_end = node_end > N ? N : node_end;

  const u32x4* lrp = desc + ((size_t)b * NCHUNK + t) * 2;
  u32x4 lv = lrp[0];
  uint32_t llast = lrp[1][0];
  u32x4 wb[4][2], wv[4][2];
  {
    u32x4 tmp[4];
#pragma unroll
    for (int k = 0; k < 4; ++k) tmp[k] = wfrag[t + 256 * k];
#pragma unroll
    for (int k = 0; k < 4; ++k) wau[t + 256 * k] = wfrag[2 * 512 + t + 256 * k];
#pragma unroll
    for (int k = 0; k < 4; ++k) recs[t + 256 * k] = tmp[k];
    __syncthreads();
#pragma unroll
    for (int blk = 0; blk < 4; ++blk)
#pragma unroll
      for (int s = 0; s < 2; ++s) {
        wb[blk][s] = recs[(0 * 8 + blk * 2 + s) * 64 + lane];
        wv[blk][s] = recs[(1 * 8 + blk * 2 + s) * 64 + lane];
      }
  }
  for (uint32_t B0 = 0;; B0 += CAPREC) {
    uint32_t o[NWIN + 1];
    {
      if (B0 != 0) {
        lv = lrp[0];
        llast = lrp[1][0];
      }
#pragma unroll
      for (int k = 0; k < 4; ++k) {
        o[2 * k] = lv[k] & 0xFFFFu;
        o[2 * k + 1] = lv[k] >> 16;
      }
      o[NWIN] = llast;
    }
    uint32_t exw[NWIN], tot[NWIN];
    {
      uint32_t inc[NWIN];
#pragma unroll
      for (int k = 0; k < NWIN; ++k) inc[k] = wave_incl_scan(o[k + 1] - o[k], lane);
      if (lane == 63) {
#pragma unroll
        for (int k = 0; k < NWIN; ++k) wsum[k][wave] = inc[k];
      }
      __syncthreads();
#pragma unroll
      for (int k = 0; k < NWIN; ++k) {
        uint32_t pre = 0, tt = 0;
#pragma unroll
        for (int j = 0; j < 4; ++j) {
          const uint32_t sv = wsum[k][j];
          if (j < wave) pre += sv;
          tt += sv;
        }
        exw[k] = pre + inc[k] - (o[k + 1] - o[k]);
        tot[k] = tt;
      }
    }
    uint32_t total = 0;
#pragma unroll
    for (int k = 0; k < NWIN; ++k) total += tot[k];
    segtab[t] = (u32x2){o[0], o[NWIN] - o[0]};

    const bool first = (B0 == 0);
    const bool last = (B0 + CAPREC >= total);
    const uint32_t B1 = B0 + CAPREC;
    uint32_t cntw[NWIN], lwoff[NWIN];
    {
      uint32_t wo = 0, lo = 0;
#pragma unroll
      for (int k = 0; k < NWIN; ++k) {
        const uint32_t ws0 = wo > B0 ? wo : B0;
        const uint32_t we = wo + tot[k];
        const uint32_t ws1 = we < B1 ? we : B1;
        cntw[k] = ws1 > ws0 ? ws1 - ws0 : 0u;
        lwoff[k] = lo;
        lo += (cntw[k] + 31u) & ~31u;
        dtab[k * 256 + t] = wo + exw[k] - (o[k] - o[0]) - ws0 + lwoff[k];
        wo = we;
      }
      if (t < NWIN) {
        uint32_t lwo = 0, cw = 0;
#pragma unroll
        for (int k = 0; k < NWIN; ++k)
          if (t == k) { lwo = lwoff[k]; cw = cntw[k]; }
        wtab[t] = (u32x2){lwo, cw};
      }
      if (t == 0) wctr = 0;
    }
    __syncthreads();
    {
      auto place = [&](const u32x4 rec, uint32_t c, uint32_t k) {
        const uint32_t dl = rec[3] - (uint32_t)nodebase;
        const uint32_t wn = magicW ? __umulhi(dl, magicW) : dl / (uint32_t)WS;
        const uint32_t wc = wn < (uint32_t)NWIN ? wn : 0u;
        const uint32_t lp = dtab[wc * 256 + c] + k;
        const u32x2 wt = wtab[wc];
        if ((lp - wt[0]) < wt[1]) {
          recs[lp] = rec;
          dlw8[lp] = (uint8_t)(dl - wc * (uint32_t)WS);
        }
      };
#pragma unroll 1
      for (int pass = 0; pass < NCHUNK / 32; pass += PBU) {
        u32x4 r0[PBU], r1[PBU];
        uint32_t ll[PBU];
        const uint32_t k0 = (uint32_t)(t & 7);
#pragma unroll
        for (int u = 0; u < PBU; ++u) {
          const int c = 32 * (pass + u) + (t >> 3);
          const u32x2 sg = segtab[c];
          ll[u] = sg[1];
          const u32x4* gp = grec + (size_t)c * CH + sg[0];
          r0[u] = gp[k0 < sg[1] ? k0 : 0u];
          r1[u] = gp[k0 + 8 < sg[1] ? k0 + 8 : 0u];
        }
#pragma unroll
        for (int u = 0; u < PBU; ++u) {
          const uint32_t c = (uint32_t)(32 * (pass + u) + (t >> 3));
          if (k0 < ll[u]) place(r0[u], c, k0);
          if (k0 + 8 < ll[u]) place(r1[u], c, k0 + 8);
          for (uint32_t k = k0 + 16; k < ll[u]; k += 8) {
            const u32x2 sg = segtab[c];
            place(grec[(size_t)c * CH + sg[0] + k], c, k);
          }
        }
      }
      {
        const u32x2 wt = wtab[t >> 5];
        const uint32_t pos = wt[1] + (uint32_t)(t & 31);
        if (pos < ((wt[1] + 31u) & ~31u)) {
          recs[wt[0] + pos] = (u32x4){0u, 0u, 0u, 0x40000000u};
          dlw8[wt[0] + pos] = 255u;
        }
      }
    }
    __syncthreads();

    for (;;) {
      uint32_t wq = 0;
      if (lane == 0) wq = atomicAdd(&wctr, 1u);
      const int win = __builtin_amdgcn_readfirstlane((int)wq);
      if (win >= NWIN) break;
      const int n0 = nodebase + win * WS;
      int wsize = node_end - n0;
      wsize = wsize > WS ? WS : wsize;
      if (wsize <= 0) continue;
      const u32x2 wtw = wtab[win];
      const uint32_t base = __builtin_amdgcn_readfirstlane(wtw[0]);
      const uint32_t cnt = __builtin_amdgcn_readfirstlane(wtw[1]);

      const int ntiles = (int)((cnt + 31u) >> 5);
      auto fetch = [&](int tt, TileIn& T) {
#pragma unroll
        for (int s2 = 0; s2 < 2; ++s2) {
          const uint32_t sn = recs[base + 32u * tt + 16u * s2 + i][2];
          const u32x4* xp = reinterpret_cast<const u32x4*>(x + (size_t)sn * 64 + 8 * q);
          T.xg[s2][0] = xp[0];
          T.xg[s2][1] = xp[4];
        }
      };

      TileIn TA, TB;
      if (ntiles > 0) fetch(0, TA);

      const int nrow = n0 + (i < wsize ? i : wsize - 1);
      const u32x4* xrow = reinterpret_cast<const u32x4*>(x + (size_t)nrow * 64 + 8 * q);
      u32x4 bsel[4];
      {
        const u32x4 xa0 = xrow[0], xa1 = xrow[4];
#pragma unroll
        for (int blk = 0; blk < 4; ++blk) {
          f32x4 a = {0.f, 0.f, 0.f, 0.f};
          a = mfma16(xa0, wau[(blk * 2 + 0) * 64 + lane], a);
          a = mfma16(xa1, wau[(blk * 2 + 1) * 64 + lane], a);
          const u32x2 wcf = wcfrag[blk * 64 + lane];
          bsel[blk] = (u32x4){pack_bf16(a[0], a[1]), pack_bf16(a[2], a[3]), wcf[0], wcf[1]};
        }
      }

      f32x4 den[4], num[4];
#pragma unroll
      for (int blk = 0; blk < 4; ++blk) {
        den[blk] = (f32x4){0.f, 0.f, 0.f, 0.f};
        num[blk] = (f32x4){0.f, 0.f, 0.f, 0.f};
      }
      if (!first) {
#pragma unroll
        for (int blk = 0; blk < 4; ++blk)
#pragma unroll
          for (int r = 0; r < 4; ++r) {
            const int nl = 4 * q + r;
            if (nl < wsize) {
              const float* pp = part + (size_t)(n0 + nl) * 128 + 4 * i + blk;
              den[blk][r] = pp[0];
              num[blk][r] = pp[64];
            }
          }
      }

      const uint32_t qm = (q == 0) ? 0xFFFFFFFFu : 0u;

      auto compute = [&](const TileIn& T, int tt) {
        u32x4 asel[2];
#pragma unroll
        for (int s2 = 0; s2 < 2; ++s2) {
          const u32x4 rec = recs[base + 32u * tt + 16u * s2 + i];
          const uint32_t u = rec[3] - (uint32_t)n0 - 4u * q;
          const uint32_t one = 0x3F80u << ((u & 1u) << 4);
          asel[s2] = (u32x4){(u >> 1) == 0u ? one : 0u, (u >> 1) == 1u ? one : 0u,
                             rec[0] & qm, rec[1] & qm};
        }
        u32x4 ar = {0u, 0u, 0u, 0u};
#pragma unroll
        for (int s2 = 0; s2 < 2; ++s2) {
          const uint32_t pv =
              *reinterpret_cast<const uint32_t*>(&dlw8[base + 32u * tt + 16u * s2 + 4u * q]);
#pragma unroll
          for (int jj = 0; jj < 4; ++jj) {
            const bool m = ((pv >> (8 * jj)) & 0xFFu) == (uint32_t)i;
            ar[2 * s2 + (jj >> 1)] |= m ? (0x3F80u << (16 * (jj & 1))) : 0u;
          }
        }
#pragma unroll
        for (int blk = 0; blk < 4; ++blk) {
          const f32x4 zero = {0.f, 0.f, 0.f, 0.f};
          f32x4 g[2], v[2];
#pragma unroll
          for (int s2 = 0; s2 < 2; ++s2) {
            g[s2] = mfma16(T.xg[s2][0], wb[blk][0], zero);
            v[s2] = mfma16(T.xg[s2][0], wv[blk][0], zero);
          }
#pragma unroll
          for (int s2 = 0; s2 < 2; ++s2) {
            g[s2] = mfma16(T.xg[s2][1], wb[blk][1], g[s2]);
            v[s2] = mfma16(T.xg[s2][1], wv[blk][1], v[s2]);
          }
#pragma unroll
          for (int s2 = 0; s2 < 2; ++s2) g[s2] = mfma16(asel[s2], bsel[blk], g[s2]);
          float ex[8], eta[8];
#pragma unroll
          for (int k = 0; k < 8; ++k)
            ex[k] = __builtin_amdgcn_exp2f(g[k >> 2][k & 3] * -1.44269504088896f);
#pragma unroll
          for (int k = 0; k < 8; ++k) eta[k] = __builtin_amdgcn_rcpf(1.f + ex[k]);
          u32x4 eb, pb;
#pragma unroll
          for (int k = 0; k < 4; ++k) {
            eb[k] = pack_bf16(eta[2 * k], eta[2 * k + 1]);
            pb[k] = pack_bf16(eta[2 * k] * v[k >> 1][(2 * k) & 3], eta[2 * k + 1] * v[k >> 1][(2 * k + 1) & 3]);
          }
          den[blk] = mfma16(ar, eb, den[blk]);
          num[blk] = mfma16(ar, pb, num[blk]);
        }
      };

      for (int tt = 0; tt < ntiles; tt += 2) {
        if (tt + 1 < ntiles) fetch(tt + 1, TB);
        compute(TA, tt);
        if (tt + 1 >= ntiles) break;
        if (tt + 2 < ntiles) fetch(tt + 2, TA);
        compute(TB, tt + 1);
      }

      if (last) {
        f32x4 uu[4];
        const u32x4 xb0 = xrow[0], xb1 = xrow[4];
#pragma unroll
        for (int blk = 0; blk < 4; ++blk) {
          f32x4 a = {0.f, 0.f, 0.f, 0.f};
          a = mfma16(xb0, wau[(8 + blk * 2 + 0) * 64 + lane], a);
          a = mfma16(xb1, wau[(8 + blk * 2 + 1) * 64 + lane], a);
          uu[blk] = a;
        }
#pragma unroll
        for (int r = 0; r < 4; ++r) {
          const int nl = 4 * q + r;
          if (nl < wsize) {
            const size_t node = (size_t)(n0 + nl);
            const u32x2 xr = *reinterpret_cast<const u32x2*>(x + node * 64 + 4 * i);
            float ov[4];
#pragma unroll
            for (int blk = 0; blk < 4; ++blk) {
              const uint32_t xw = xr[blk >> 1];
              const float xv = (blk & 1) ? bf_hi(xw) : bf_lo(xw);
              float z = uu[blk][r] + num[blk][r] * __builtin_amdgcn_rcpf(den[blk][r] + 1e-6f);
              z = z > 0.f ? z : 0.f;
              ov[blk] = xv + z;
            }
            const u32x2 pk = {pack_bf16(ov[0], ov[1]), pack_bf16(ov[2], ov[3])};
            *reinterpret_cast<u32x2*>(out + node * 64 + 4 * i) = pk;
          }
        }
      } else {
#pragma unroll
        for (int blk = 0; blk < 4; ++blk)
#pragma unroll
          for (int r = 0; r < 4; ++r) {
            const int nl = 4 * q + r;
            if (nl < wsize) {
              float* pp = part + (size_t)(n0 + nl) * 128 + 4 * i + blk;
              pp[0] = den[blk][r];
              pp[64] = num[blk][r];
            }
          }
      }
    }
    if (last) break;
    __syncthreads();
  }
}

uint32_t exact_magic(int divisor, int maxval) {
  uint32_t magic = (uint32_t)((0x100000000ull / (uint64_t)divisor) + 1ull);
  for (int v = 0; v < maxval; ++v)
    if ((uint32_t)(((uint64_t)(uint32_t)v * magic) >> 32) != (uint32_t)(v / divisor)) return 0;
  return magic;
}

}

extern "C" void kernel_launch(void* const* d_in, const int* in_sizes, int n_in, void* d_out,
                              int out_size, void* d_ws, size_t ws_size, hipStream_t stream) {
  const uint16_t* x  = (const uint16_t*)d_in[0];
  const u32x2* ea    = (const u32x2*)d_in[1];
  const int* src     = (const int*)d_in[2];
  const int* dst     = (const int*)d_in[3];
  const uint16_t* wa = (const uint16_t*)d_in[4];
  const uint16_t* wb = (const uint16_t*)d_in[5];
  const uint16_t* wc = (const uint16_t*)d_in[6];
  const uint16_t* wu = (const uint16_t*)d_in[7];
  const uint16_t* wv = (const uint16_t*)d_in[8];
  const int N = in_sizes[0] / 64;
  const int E = in_sizes[2];

  const int CH = (E + NCHUNK - 1) / NCHUNK;
  const int NPB = (N + NB - 1) / NB;
  const int WS = (NPB + NWIN - 1) / NWIN;
  const uint32_t magicB = exact_magic(NPB, N);
  const uint32_t magicW = exact_magic(WS, NPB + 1);

  char* ws = (char*)d_ws;
  size_t off = 0;
  u32x4* grec = (u32x4*)(ws + off);        off += (size_t)E * 16;
  off = (off + 255) & ~(size_t)255;
  u32x4* desc = (u32x4*)(ws + off);        off += (size_t)NB * NCHUNK * 32;
  off = (off + 255) & ~(size_t)255;
  u32x4* wfrag = (u32x4*)(ws + off);       off += 4 * 8 * 64 * 16;
  u32x2* wcfrag = (u32x2*)(ws + off);      off += 4 * 64 * 8;
  off = (off + 255) & ~(size_t)255;
  float* part = (float*)(ws + off);

  k1_sort<<<NCHUNK + NPACKWG, 256, 0, stream>>>(src, dst, ea, grec, desc, wa, wb, wc, wu, wv,
                                                wfrag, wcfrag, E, CH, NPB, WS, magicB, magicW);
  k2_main<<<NB, 256, 0, stream>>>(x, grec, desc, wfrag, wcfrag, (uint16_t*)d_out, part, N, CH,
                                  NPB, WS, magicW);
}
```
